```python
import torch

def _softmax(x, axis=-1):
    return torch.softmax(x, dim=axis)

def _gelu(x):
    return torch.nn.functional.gelu(x, approximate='tanh')

def _silu(x):
    return torch.nn.functional.silu(x)

def _sigmoid(x):
    return torch.sigmoid(x)

def _layernorm(x, axis=-1, eps=1e-05):
    m = x.mean(dim=axis, keepdim=True)
    v = x.var(dim=axis, keepdim=True, unbiased=False)
    return (x - m) / torch.sqrt(v + eps)

def _scatter_add(tgt, idx, src, axis=0):
    return tgt.clone().index_add_(axis, idx, src)

def _segment_sum(src, seg, n):
    out = torch.zeros((n,) + src.shape[1:], dtype=src.dtype)
    return out.index_add_(0, seg, src)

def _pad(x, pw):
    flat = tuple((v for pair in reversed(pw) for v in pair))
    return torch.nn.functional.pad(x, flat)
kernel_name = 'gated_mix__grouped_kv_mqa__mixer_gmlp__prompt'

def setup_inputs():

    _w = lambda a: (lambda t: t.to(torch.bfloat16) if t.is_floating_point() else t)(torch.as_tensor(a))

    def _A_setup():
        B, S, D, H = (1, 2048, 576, 32)
        Dh = D // H
        return {'x': _w((lambda *s: torch.randn(*s, dtype=torch.bfloat16) * 0.05)(B, S, D)), 'wq': _w((lambda *s: torch.randn(*s, dtype=torch.bfloat16) * 3.2)(D, H * Dh)), 'wk': _w((lambda *s: torch.randn(*s, dtype=torch.bfloat16) * 3.2)(D, Dh)), 'wv': _w((lambda *s: torch.randn(*s, dtype=torch.bfloat16) * 0.4)(D, Dh)), 'wo': _w((lambda *s: torch.randn(*s, dtype=torch.bfloat16) * 0.4)(H * Dh, D))}
    _A_in = _A_setup()

    def _B_setup():
        B, S, D, F = (1, 2048, 576, 4608)
        Fh = F // 2
        return {'x': _w((lambda *s: torch.randn(*s, dtype=torch.bfloat16) * 0.05)(B, S, D)), 'w_in': _w((lambda *s: torch.randn(*s, dtype=torch.bfloat16) * 1.6)(D, 2 * Fh)), 'w_sp': _w(0.01 * (lambda *s: torch.randn(*s, dtype=torch.bfloat16) * 1.6)(S, S)), 'b_sp': _w(1.0 + 0.01 * (lambda *s: torch.randn(*s, dtype=torch.bfloat16) * 0.1)(S)), 'w_out': _w((lambda *s: torch.randn(*s, dtype=torch.bfloat16) * 0.1)(Fh, D))}
    _B_in = _B_setup()
    return {'A_x': _A_in['x'], 'A_wq': _A_in['wq'], 'A_wk': _A_in['wk'], 'A_wv': _A_in['wv'], 'A_wo': _A_in['wo'], 'B_x': _B_in['x'], 'B_w_in': _B_in['w_in'], 'B_w_sp': _B_in['w_sp'], 'B_b_sp': _B_in['b_sp'], 'B_w_out': _B_in['w_out'], 'gate_w': _w((lambda *s: torch.randn(*s, dtype=torch.bfloat16) * 0.1)(576) * 16.0)}

def reference(A_x, A_wq, A_wk, A_wv, A_wo, B_x, B_w_in, B_w_sp, B_b_sp, B_w_out, gate_w):

    def _A_ref(x, wq, wk, wv, wo):
        B, S, D = x.shape
        H = 32
        Dh = D // H
        q = (lambda x, *d: x.permute(*d))((x @ wq).reshape(B, S, H, Dh), 0, 2, 1, 3)
        k = x @ wk
        v = x @ wv
        scores = torch.einsum('bhqd,bkd->bhqk', q, k) * Dh ** (-0.5)
        p = _softmax(scores, axis=-1)
        o = torch.einsum('bhqk,bkd->bhqd', p, v)
        return (lambda x, *d: x.permute(*d))(o, 0, 2, 1, 3).reshape(B, S, H * Dh) @ wo

    def _B_ref(x, w_in, w_sp, b_sp, w_out):
        B, S, D = x.shape
        F = w_in.shape[-1]
        Fh = F // 2
        z = _gelu(torch.einsum('bsd,df->bsf', x, w_in))
        z1 = z[..., :Fh].clone()
        z2 = _layernorm(z[..., Fh:], axis=-1)
        g = torch.einsum('ts,bsf->btf', w_sp, z2) + b_sp[None, :, None]
        return torch.einsum('bsf,fd->bsd', z1 * g, w_out)
    x = A_x
    g = _sigmoid((x * gate_w).sum(-1, keepdims=True))
    a = _A_ref(x, A_wq, A_wk, A_wv, A_wo)
    b = _B_ref(x, B_w_in, B_w_sp, B_b_sp, B_w_out)
    return g * a + (1 - g) * b
```

```cpp
#include <hip/hip_runtime.h>
#include <hip/hip_bf16.h>

typedef __attribute__((ext_vector_type(8))) __bf16 bf16x8;
typedef __attribute__((ext_vector_type(4))) short s16x4;
typedef __attribute__((ext_vector_type(8))) short s16x8;
typedef __attribute__((ext_vector_type(4))) float f32x4;
typedef __attribute__((ext_vector_type(16))) float f32x16;
typedef __attribute__((ext_vector_type(4))) unsigned u32x4;
typedef __attribute__((ext_vector_type(2))) unsigned u32x2;
typedef unsigned short u16;
#define LDS_AS __attribute__((address_space(3)))
#define GLB_AS __attribute__((address_space(1)))
#define DEVI __device__ __forceinline__

constexpr int S_ = 2048, D_ = 576, FH = 2304, NKB = 90;
constexpr size_t WS_EPOCH = 0;
constexpr size_t WS_STATS = 4096;
constexpr size_t WS_GATE = WS_STATS + 16 * 16 * 128 * 16;
constexpr size_t WS_KV = WS_GATE + 8192;
constexpr size_t WS_Q = WS_KV + 8 * 25600;
constexpr size_t WS_Z2 = WS_Q + (size_t)S_ * D_ * 2;
constexpr size_t WS_U = WS_Z2 + (size_t)S_ * FH * 2;
constexpr size_t WS_WC = WS_U + (size_t)128 * NKB * 1024;

DEVI unsigned cvtpk(float lo, float hi) {
  typedef __attribute__((ext_vector_type(2))) __bf16 bf16x2;
  bf16x2 v; v[0] = (__bf16)lo; v[1] = (__bf16)hi;
  return __builtin_bit_cast(unsigned, v);
}
DEVI float bflo(unsigned u) { return __uint_as_float(u << 16); }
DEVI float bfhi(unsigned u) { return __uint_as_float(u & 0xffff0000u); }
DEVI float bf2f(u16 u) { return __uint_as_float((unsigned)u << 16); }
DEVI void glds16(const void* sbase, int voff, unsigned lds) {
  unsigned keep;
  asm volatile("s_mov_b32 %0, m0\n\ts_mov_b32 m0, %3\n\ts_nop 0\n\tglobal_load_lds_dwordx4 %1, %2\n\ts_mov_b32 m0, %0"
               : "=&s"(keep) : "v"(voff), "s"(sbase), "s"(lds) : "memory");
}
DEVI unsigned lds_addr(const char* p) { return (unsigned)(size_t)(const LDS_AS char*)p; }
DEVI bf16x8 lds_frag(const char* p) { return *(const bf16x8*)p; }
DEVI bf16x8 lds_tr_frag(const char* p0, const char* p1) {
  s16x4 a = __builtin_amdgcn_ds_read_tr16_b64_v4i16((LDS_AS s16x4*)p0);
  s16x4 b = __builtin_amdgcn_ds_read_tr16_b64_v4i16((LDS_AS s16x4*)p1);
  s16x8 r = __builtin_shufflevector(a, b, 0, 1, 2, 3, 4, 5, 6, 7);
  return __builtin_bit_cast(bf16x8, r);
}
#define MFMA16(a, b, c) __builtin_amdgcn_mfma_f32_16x16x32_bf16(a, b, c, 0, 0, 0)
#define MFMA32(a, b, c) __builtin_amdgcn_mfma_f32_32x32x16_bf16(a, b, c, 0, 0, 0)
template <int N> DEVI void wait_vm() { asm volatile("s_waitcnt vmcnt(%0)" ::"i"(N) : "memory"); }
DEVI void wait_lgkm0() { asm volatile("s_waitcnt lgkmcnt(0)" ::: "memory"); }
DEVI void barrier() { __builtin_amdgcn_s_barrier(); asm volatile("" ::: "memory"); }
DEVI float gelu(float x) {
  const float c0 = -2.0f * 0.7978845608028654f * 1.4426950408889634f, c1 = c0 * 0.044715f;
  float w = x * __builtin_fmaf(x * x, c1, c0);
  return x * __builtin_amdgcn_rcpf(1.0f + __builtin_amdgcn_exp2f(w));
}
DEVI int swzf(int r) { return ((r >> 1) & 1) | (((r >> 3) & 3) << 1); }
DEVI int swap23(int r) { return (r & ~12) | ((r & 4) << 1) | ((r & 8) >> 1); }

constexpr int K1_SLOT = 34816, K1_LDS = 4 * K1_SLOT + 1024;

__global__ __launch_bounds__(256, 1) void k1_proj(const u16* __restrict__ x, const u16* __restrict__ wq,
                                                  const u16* __restrict__ wk, const u16* __restrict__ wv,
                                                  const u16* __restrict__ win, const u16* __restrict__ gw,
                                                  const u16* __restrict__ wout, const u16* __restrict__ wo,
                                                  char* __restrict__ ws) {
  extern __shared__ __attribute__((aligned(16))) char sm[];
  const int lane = threadIdx.x & 63, w = __builtin_amdgcn_readfirstlane(threadIdx.x >> 6);
  const int xcd = blockIdx.x & 7, idx = blockIdx.x >> 3;
  const int rb = (xcd & 1) * 8 + (idx & 7), j = (xcd >> 1) * 4 + (idx >> 3);
  const int li = lane & 15, g = lane >> 4;
  GLB_AS unsigned* epoch_p = (GLB_AS unsigned*)(ws + WS_EPOCH + rb * 128);
  const unsigned epoch = __builtin_amdgcn_readfirstlane(*epoch_p);
  const unsigned lds0 = lds_addr(sm);

  int xoff[4], woff[5];
#pragma unroll
  for (int q = 0; q < 4; ++q) {
    int row = 32 * w + 8 * q + (lane >> 3);
    xoff[q] = (rb * 128 + row) * (D_ * 2) + (((lane & 7) ^ swzf(row)) << 4);
  }
#pragma unroll
  for (int p = 0; p < 5; ++p) {
    int c = 64 * (w + 4 * p) + lane, rho = c / 18, cc = c - rho * 18;
    woff[p] = swap23(rho) * (2 * FH * 2) + cc * 16;
  }
  const char* xb = (const char*)x;
  auto issue = [&](int s) {
    const unsigned slot = lds0 + (s & 3) * K1_SLOT;
    if (s < 9) {
#pragma unroll
      for (int q = 0; q < 4; ++q) glds16(xb + s * 128, xoff[q], slot + (4 * w + q) * 1024);
    }
    if (s < 18) {
      const char* wb = (const char*)win + (size_t)(s < 9 ? s : s - 9) * 64 * (2 * FH * 2) + ((s < 9 ? FH : 0) + 144 * j) * 2;
#pragma unroll
      for (int p = 0; p < 5; ++p)
        if (p < 4 || w < 2) glds16(wb, woff[p], slot + 16384 + (w + 4 * p) * 1024);
    } else if (j < 12) {
      const char* wb = (const char*)wq + (size_t)(s - 18) * 192 * (D_ * 2) + 96 * j;
#pragma unroll
      for (int p = 0; p < 5; ++p) {
        int c = 64 * (w + 4 * p) + lane, rho = c / 6, cc = c - rho * 6;
        if (p < 4 || w < 2) glds16(wb, swap23(rho) * (D_ * 2) + cc * 16, slot + 16384 + (w + 4 * p) * 1024);
      }
    } else if (j < 14 && s == 18) {
      const char* wb = (const char*)(j == 12 ? wk : wv);
#pragma unroll
      for (int p = 0; p < 6; ++p) {
        int c = 64 * (w + 4 * p) + lane;
        if (p < 5 || w == 0) glds16(wb, min(c, 1295) * 16, slot + (w + 4 * p) * 1024);
      }
    } else if (j == 14 && s == 18) {
      if (w < 2) glds16(gw, min(64 * w + lane, 71) * 16, slot + w * 1024);
    }
  };
  issue(0); issue(1); issue(2);
  {
    const int item = blockIdx.x * 256 + threadIdx.x;
    if (item < NKB * 4 * 144) {
      const int kg = item / 144, dq = item - kg * 144, kb = kg >> 2, gg = kg & 3;
      u32x2 v[8];
#pragma unroll
      for (int e = 0; e < 8; ++e) {
        const u16* rowp;
        if (kb < 72) {
          const int m = kb / 9, r = kb - 9 * m;
          const int f = r < 8 ? 144 * (2 * m + (r >> 2)) + 16 * (2 * (r & 3) + (e >> 2)) + 4 * gg + (e & 3)
                              : 144 * (2 * m + (e >> 2)) + 128 + 4 * gg + (e & 3);
          rowp = wout + f * D_;
        } else {
          const int c = kg - 288;
          const int ko = c < 64 ? (c & 31) * 18 + 8 * (e >> 2) + 4 * (c >> 5) + (e & 3) : (4 * (c - 64) + (e >> 1)) * 18 + 16 + (e & 1);
          rowp = wo + ko * D_;
        }
        v[e] = *(const u32x2*)(rowp + 4 * dq);
      }
      char* dst = ws + WS_WC + ((size_t)((dq >> 2) * NKB + kb) * 64 + gg * 16 + 4 * (dq & 3)) * 16;
#pragma unroll
      for (int d = 0; d < 4; ++d) {
        u32x4 o;
#pragma unroll
        for (int q = 0; q < 4; ++q)
          o[q] = __builtin_amdgcn_perm(v[2 * q + 1][d >> 1], v[2 * q][d >> 1], (d & 1) ? 0x07060302u : 0x05040100u);
        *(u32x4*)(dst + d * 16) = o;
      }
    }
  }

  bf16x8 xr[2][18];
  f32x4 acc0[9][2], acc1[9][2];
  u32x4 z2p[9];
  float ssum[8], ssq[8];
#pragma unroll
  for (int i = 0; i < 8; ++i) { ssum[i] = 0.f; ssq[i] = 0.f; }
#pragma unroll
  for (int n = 0; n < 9; ++n)
#pragma unroll
    for (int b = 0; b < 2; ++b) { acc0[n][b] = f32x4{0, 0, 0, 0}; acc1[n][b] = f32x4{0, 0, 0, 0}; }

  const int xrow = 32 * w + 8 * (li >> 2) + (li & 3);
  const int xrd = xrow * 128, xsw = swzf(xrow);
  const int trrow = 16 * (g >> 1) + 4 * (g & 1) + (li >> 2);
  const int tr144 = 16384 + trrow * 288 + 8 * (li & 3);
  const int tr48 = 16384 + trrow * 96 + 8 * (li & 3);
  char* Ub = ws + WS_U;
  const int tb0 = (rb * 4 + w) * 2;

#pragma unroll
  for (int s = 0; s < 9; ++s) {
    if (s < 7) wait_vm<16>(); else if (s == 7) wait_vm<12>(); else wait_vm<8>();
    wait_lgkm0(); barrier();
    issue(s + 3);
    const char* slot = sm + (s & 3) * K1_SLOT;
#pragma unroll
    for (int ks = 0; ks < 2; ++ks) {
#pragma unroll
      for (int b = 0; b < 2; ++b) xr[b][2 * s + ks] = lds_frag(slot + xrd + b * 512 + (((4 * ks + g) ^ xsw) << 4));
#pragma unroll
      for (int n = 0; n < 9; ++n) {
        const char* p = slot + tr144 + ks * 32 * 288 + n * 32;
        bf16x8 wf = lds_tr_frag(p, p + 8 * 288);
#pragma unroll
        for (int b = 0; b < 2; ++b) acc0[n][b] = MFMA16(xr[b][2 * s + ks], wf, acc0[n][b]);
      }
    }
  }
#pragma unroll
  for (int s = 9; s < 18; ++s) {
    if (s < 16) wait_vm<8>();
    else if (j < 12) wait_vm<8>();
    else if (s == 16) wait_vm<4>();
    else wait_vm<0>();
    wait_lgkm0(); barrier();
    issue(s + 3);
    const char* slot = sm + (s & 3) * K1_SLOT;
    const int e = s - 9;
#pragma unroll
    for (int ks = 0; ks < 2; ++ks) {
#pragma unroll
      for (int n = 0; n < 9; ++n) {
        const char* p = slot + tr144 + ks * 32 * 288 + n * 32;
        bf16x8 wf = lds_tr_frag(p, p + 8 * 288);
#pragma unroll
        for (int b = 0; b < 2; ++b) acc1[n][b] = MFMA16(wf, xr[b][2 * e + ks], acc1[n][b]);
      }
      unsigned p0 = cvtpk(gelu(acc0[e][ks][0]), gelu(acc0[e][ks][1])), p1 = cvtpk(gelu(acc0[e][ks][2]), gelu(acc0[e][ks][3]));
      z2p[e][2 * ks] = p0; z2p[e][2 * ks + 1] = p1;
      float v[4] = {bflo(p0), bfhi(p0), bflo(p1), bfhi(p1)};
#pragma unroll
      for (int r = 0; r < 4; ++r) { ssum[4 * ks + r] += v[r]; ssq[4 * ks + r] = __builtin_fmaf(v[r], v[r], ssq[4 * ks + r]); }
      asm volatile("" : "+v"(ssum[4 * ks]), "+v"(ssum[4 * ks + 1]), "+v"(ssum[4 * ks + 2]), "+v"(ssum[4 * ks + 3]),
                        "+v"(ssq[4 * ks]), "+v"(ssq[4 * ks + 1]), "+v"(ssq[4 * ks + 2]), "+v"(ssq[4 * ks + 3]));
    }
  }
  const unsigned tag = epoch + 0x9E3779B9u;
  {
#pragma unroll
    for (int i = 0; i < 8; ++i) {
#pragma unroll
      for (int m = 1; m < 16; m <<= 1) { ssum[i] += __shfl_xor(ssum[i], m); ssq[i] += __shfl_xor(ssq[i], m); }
    }
    float a = 0.f;
#pragma unroll
    for (int i = 0; i < 8; ++i) { if ((li >> 1) == i) a = (li & 1) ? ssq[i] : ssum[i]; }
    GLB_AS unsigned long long* st = (GLB_AS unsigned long long*)(ws + WS_STATS + ((size_t)(rb * 16 + j) * 128 + 32 * w) * 16);
    __hip_atomic_store(st + lane, ((unsigned long long)tag << 32) | __float_as_uint(a), __ATOMIC_RELAXED, __HIP_MEMORY_SCOPE_AGENT);
  }
  auto z1_store = [&](int i) {
#pragma unroll
    for (int b = 0; b < 2; ++b) {
      if (i < 4) {
        u32x4 o;
        o[0] = cvtpk(gelu(acc1[2 * i][b][0]), gelu(acc1[2 * i][b][1])); o[1] = cvtpk(gelu(acc1[2 * i][b][2]), gelu(acc1[2 * i][b][3]));
        o[2] = cvtpk(gelu(acc1[2 * i + 1][b][0]), gelu(acc1[2 * i + 1][b][1])); o[3] = cvtpk(gelu(acc1[2 * i + 1][b][2]), gelu(acc1[2 * i + 1][b][3]));
        *(u32x4*)(Ub + ((size_t)(tb0 + b) * NKB + 9 * (j >> 1) + 4 * (j & 1) + i) * 1024 + lane * 16) = o;
      } else {
        u32x2 o;
        o[0] = cvtpk(gelu(acc1[8][b][0]), gelu(acc1[8][b][1])); o[1] = cvtpk(gelu(acc1[8][b][2]), gelu(acc1[8][b][3]));
        *(u32x2*)(Ub + ((size_t)(tb0 + b) * NKB + 9 * (j >> 1) + 8) * 1024 + lane * 16 + (j & 1) * 8) = o;
      }
    }
  };
  const int tok0 = rb * 128 + 32 * w;
  if (j < 12) {
    f32x4 a2[3][2];
#pragma unroll
    for (int n = 0; n < 3; ++n) { a2[n][0] = f32x4{0, 0, 0, 0}; a2[n][1] = f32x4{0, 0, 0, 0}; }
#pragma unroll
    for (int s = 18; s < 21; ++s) {
      if (s == 18) wait_vm<8>(); else if (s == 19) wait_vm<4>(); else wait_vm<0>();
      wait_lgkm0(); barrier();
      const char* slot = sm + (s & 3) * K1_SLOT;
#pragma unroll
      for (int ks = 0; ks < 6; ++ks)
#pragma unroll
        for (int n = 0; n < 3; ++n) {
          const char* p = slot + tr48 + ks * 32 * 96 + n * 32;
          bf16x8 wf = lds_tr_frag(p, p + 8 * 96);
#pragma unroll
          for (int b = 0; b < 2; ++b) a2[n][b] = MFMA16(wf, xr[b][6 * (s - 18) + ks], a2[n][b]);
        }
      if (s == 18) { z1_store(0); z1_store(4); } else if (s == 19) { z1_store(1); z1_store(2); } else z1_store(3);
    }
    const float qs = 0.23570226039551584f * 1.4426950408889634f;
#pragma unroll
    for (int n = 0; n < 3; ++n)
#pragma unroll
      for (int b = 0; b < 2; ++b) {
        u32x2 o; o[0] = cvtpk(a2[n][b][0] * qs, a2[n][b][1] * qs); o[1] = cvtpk(a2[n][b][2] * qs, a2[n][b][3] * qs);
        int tok = tok0 + 8 * (li >> 2) + 4 * b + (li & 3);
        *(u32x2*)(ws + WS_Q + (size_t)tok * (D_ * 2) + (48 * j + 16 * n + 4 * g) * 2) = o;
      }
  } else {
#pragma unroll
    for (int i = 0; i < 5; ++i) z1_store(i);
    const int tile = rb * 4 + w;
    char* kvb = ws + WS_KV + (tile >> 3) * 25600;
    if (j < 14) {
      const u16* wl = (const u16*)(sm + (18 & 3) * K1_SLOT);
      f32x4 a2[2][2];
#pragma unroll
      for (int n = 0; n < 2; ++n) { a2[n][0] = f32x4{0, 0, 0, 0}; a2[n][1] = f32x4{0, 0, 0, 0}; }
#pragma unroll
      for (int ks = 0; ks < 18; ++ks)
#pragma unroll
        for (int n = 0; n < 2; ++n) {
          s16x8 t;
          const int col = n == 0 ? li : 16 + (li & 1);
#pragma unroll
          for (int e = 0; e < 8; ++e) t[e] = (short)wl[(32 * ks + 8 * g + e) * 18 + col];
          bf16x8 wf = __builtin_bit_cast(bf16x8, t);
#pragma unroll
          for (int b = 0; b < 2; ++b) {
            if (j == 12) a2[n][b] = MFMA16(wf, xr[b][ks], a2[n][b]);
            else a2[n][b] = MFMA16(xr[b][ks], wf, a2[n][b]);
          }
        }
#pragma unroll
      for (int b = 0; b < 2; ++b) {
        u32x2 o0; o0[0] = cvtpk(a2[0][b][0], a2[0][b][1]); o0[1] = cvtpk(a2[0][b][2], a2[0][b][3]);
        u32x2 o1; o1[0] = cvtpk(a2[1][b][0], a2[1][b][1]); o1[1] = cvtpk(a2[1][b][2], a2[1][b][3]);
        if (j == 12) {
          const int key = 8 * (li >> 2) + 4 * b + (li & 3);
          *(u32x2*)(kvb + (tile & 7) * 1024 + ((g >> 1) * 32 + key) * 16 + (g & 1) * 8) = o0;
          if (g == 0) *(unsigned*)(kvb + 24576 + (tile & 7) * 128 + key * 4) = o1[0];
        } else {
          char* vb = kvb + 8192 + (tile & 7) * 2048 + (g >> 1) * 1024 + b * 512 + (g & 1) * 8;
          *(u32x2*)(vb + li * 16) = o0;
          if (li >= 2) { unsigned c = (li == 2 || li == 6) ? 0x3F803F80u : 0u; o1[0] = c; o1[1] = c; }
          *(u32x2*)(vb + (16 + li) * 16) = o1;
        }
      }
    } else if (j == 14) {
      f32x4 a2[2] = {f32x4{0, 0, 0, 0}, f32x4{0, 0, 0, 0}};
#pragma unroll
      for (int ks = 0; ks < 18; ++ks) {
        bf16x8 wf = lds_frag(sm + (18 & 3) * K1_SLOT + 64 * ks + 16 * g);
        a2[0] = MFMA16(wf, xr[0][ks], a2[0]); a2[1] = MFMA16(wf, xr[1][ks], a2[1]);
      }
      if (g < 2) {
        float v = g == 0 ? a2[0][0] : a2[1][0];
        ((float*)(ws + WS_GATE))[tok0 + 8 * (li >> 2) + 4 * g + (li & 3)] = 1.0f / (1.0f + __expf(-v));
      }
    }
  }
  {
    const GLB_AS unsigned long long* st = (const GLB_AS unsigned long long*)(ws + WS_STATS + ((size_t)(rb * 16) * 128 + 32 * w) * 16) + lane;
    float tot;
    for (int spin = 0; spin < (1 << 22); ++spin) {
      bool ok = true; tot = 0.f;
#pragma unroll
      for (int p = 0; p < 16; ++p) {
        unsigned long long v = __hip_atomic_load(st + p * 256, __ATOMIC_RELAXED, __HIP_MEMORY_SCOPE_AGENT);
        ok &= (unsigned)(v >> 32) == tag; tot += __uint_as_float((unsigned)v);
      }
      if (__all(ok)) break;
      __builtin_amdgcn_s_sleep(2);
    }
    float other = __shfl_xor(tot, 1);
    float sum = (lane & 1) ? other : tot, sq = (lane & 1) ? tot : other;
    float mean = sum * (1.0f / FH);
    float var = fmaxf(sq * (1.0f / FH) - mean * mean, 0.f);
    float rstd = 1.0f / sqrtf(var + 1e-5f);
    float mine = (lane & 1) ? -mean * rstd : rstd;
    float rs[8], nm[8];
#pragma unroll
    for (int i = 0; i < 8; ++i) { rs[i] = __shfl(mine, 16 * g + 2 * i); nm[i] = __shfl(mine, 16 * g + 2 * i + 1); }
    char* zb = ws + WS_Z2 + ((size_t)(j * 64 + rb * 4 + w) * 9) * 1024 + lane * 16;
#pragma unroll
    for (int n = 0; n < 9; ++n) {
      u32x4 o;
#pragma unroll
      for (int q = 0; q < 4; ++q)
        o[q] = cvtpk(__builtin_fmaf(bflo(z2p[n][q]), rs[2 * q], nm[2 * q]), __builtin_fmaf(bfhi(z2p[n][q]), rs[2 * q + 1], nm[2 * q + 1]));
      *(u32x4*)(zb + n * 1024) = o;
    }
  }
  if (j == 0) {
    __syncthreads();
    if (threadIdx.x == 0) __hip_atomic_store(epoch_p, epoch + 1, __ATOMIC_RELAXED, __HIP_MEMORY_SCOPE_AGENT);
  }
}

constexpr int K2_SLOT = 34816, K2_LDS = 2 * K2_SLOT, KV_CH = 25600;
typedef __attribute__((ext_vector_type(4), aligned(4))) unsigned u32x4u;

DEVI void gemm2_part(const u16* __restrict__ wsp, const u16* __restrict__ bsp, char* __restrict__ ws, char* sm, int blk) {
  const int lane = threadIdx.x & 63, w = __builtin_amdgcn_readfirstlane(threadIdx.x >> 6);
  const int xcd = blk & 7, idx = blk >> 3;
  const int rb = (xcd & 1) * 8 + (idx & 7), j = (xcd >> 1) * 4 + (idx >> 3);
  const int li = lane & 15, g = lane >> 4;
  const unsigned lds0 = lds_addr(sm);
  int boff[4];
#pragma unroll
  for (int q = 0; q < 4; ++q) {
    int row = 32 * w + 8 * q + (lane >> 3);
    boff[q] = row * (S_ * 2) + (((lane & 7) ^ swzf(row)) << 4);
  }
  const char* ab = ws + WS_Z2 + (size_t)j * 64 * 9216 + w * 1024;
  const char* bb = (const char*)wsp + (size_t)rb * 128 * (S_ * 2);
  const int l16 = lane * 16;
  auto issue = [&](int s) {
    const unsigned slot = lds0 + (s & 1) * K2_SLOT;
#pragma unroll
    for (int q = 0; q < 4; ++q) glds16(bb + s * 128, boff[q], slot + (4 * w + q) * 1024);
#pragma unroll
    for (int p = 0; p < 5; ++p)
      if (p < 4 || w < 2) glds16(ab + s * 18432 + p * 4096, l16, slot + 16384 + (w + 4 * p) * 1024);
  };
  issue(0);
  f32x4 acc[9][2];
#pragma unroll
  for (int n = 0; n < 9; ++n) { acc[n][0] = f32x4{0, 0, 0, 0}; acc[n][1] = f32x4{0, 0, 0, 0}; }
  const int xrow = 32 * w + 8 * (li >> 2) + (li & 3);
  const int xrd = xrow * 128, xsw = swzf(xrow);
  auto compute = [&](const char* slot) {
#pragma unroll
    for (int ks = 0; ks < 2; ++ks) {
      bf16x8 b0 = lds_frag(slot + xrd + (((4 * ks + g) ^ xsw) << 4)), b1 = lds_frag(slot + xrd + 512 + (((4 * ks + g) ^ xsw) << 4));
#pragma unroll
      for (int n = 0; n < 9; ++n) {
        bf16x8 a = lds_frag(slot + 16384 + (ks * 9 + n) * 1024 + l16);
        acc[n][0] = MFMA16(a, b0, acc[n][0]); acc[n][1] = MFMA16(a, b1, acc[n][1]);
      }
    }
  };
  for (int s = 0; s < 30; s += 2) {
    wait_vm<0>(); wait_lgkm0(); barrier();
    issue(s + 1); compute(sm);
    wait_vm<0>(); wait_lgkm0(); barrier();
    issue(s + 2); compute(sm + K2_SLOT);
  }
  wait_vm<0>(); wait_lgkm0(); barrier();
  issue(31); compute(sm);
  wait_vm<0>(); wait_lgkm0(); barrier();
  char* ub = ws + WS_U + ((size_t)((rb * 4 + w) * 2) * NKB + 9 * (j >> 1)) * 1024 + l16;
  u32x4 z1[2][4]; u32x2 z1o[2]; float bs[2], og[2];
#pragma unroll
  for (int b = 0; b < 2; ++b) {
#pragma unroll
    for (int i = 0; i < 4; ++i) z1[b][i] = *(const u32x4*)(ub + (b * NKB + 4 * (j & 1) + i) * 1024);
    z1o[b] = *(const u32x2*)(ub + (b * NKB + 8) * 1024 + (j & 1) * 8);
    const int tok = rb * 128 + xrow + 4 * b;
    bs[b] = bf2f(bsp[tok]); og[b] = ((const float*)(ws + WS_GATE))[tok];
  }
  asm volatile("" ::: "memory");
  compute(sm + K2_SLOT);
#pragma unroll
  for (int b = 0; b < 2; ++b) {
    const float sc = 1.0f - og[b], bo = bs[b] * sc;
    auto hp = [&](unsigned z, float a0, float a1) { return cvtpk(bflo(z) * __builtin_fmaf(a0, sc, bo), bfhi(z) * __builtin_fmaf(a1, sc, bo)); };
#pragma unroll
    for (int i = 0; i < 4; ++i) {
      u32x4 o;
      o[0] = hp(z1[b][i][0], acc[2 * i][b][0], acc[2 * i][b][1]); o[1] = hp(z1[b][i][1], acc[2 * i][b][2], acc[2 * i][b][3]);
      o[2] = hp(z1[b][i][2], acc[2 * i + 1][b][0], acc[2 * i + 1][b][1]); o[3] = hp(z1[b][i][3], acc[2 * i + 1][b][2], acc[2 * i + 1][b][3]);
      *(u32x4*)(ub + (b * NKB + 4 * (j & 1) + i) * 1024) = o;
    }
    u32x2 o; o[0] = hp(z1o[b][0], acc[8][b][0], acc[8][b][1]); o[1] = hp(z1o[b][1], acc[8][b][2], acc[8][b][3]);
    *(u32x2*)(ub + (b * NKB + 8) * 1024 + (j & 1) * 8) = o;
  }
}

DEVI bf16x8 as_frag(u32x4 v) { return __builtin_bit_cast(bf16x8, v); }
DEVI void pv_step(const f32x16& s, float sub, bf16x8 v0, bf16x8 v1, f32x16& o) {
  u32x4 p0, p1;
#pragma unroll
  for (int q = 0; q < 4; ++q) {
    p0[q] = cvtpk(__builtin_amdgcn_exp2f(s[2 * q] - sub), __builtin_amdgcn_exp2f(s[2 * q + 1] - sub));
    p1[q] = cvtpk(__builtin_amdgcn_exp2f(s[8 + 2 * q] - sub), __builtin_amdgcn_exp2f(s[9 + 2 * q] - sub));
  }
  o = MFMA32(v0, as_frag(p0), o); o = MFMA32(v1, as_frag(p1), o);
}
DEVI float max16(const f32x16& s, float m) {
#pragma unroll
  for (int r = 0; r < 16; r += 2) m = fmaxf(fmaxf(s[r], s[r + 1]), m);
  return m;
}

DEVI void attn_part(char* __restrict__ ws, char* sm, int blk) {
  const int lane = threadIdx.x & 63, w = __builtin_amdgcn_readfirstlane(threadIdx.x >> 6);
  const int head = lane & 31, h = lane >> 5, l16 = lane * 16;
  const unsigned lds0 = lds_addr(sm);
  const char* kvg = ws + WS_KV;
  auto issue = [&](int c) {
#pragma unroll
    for (int p = 0; p < 7; ++p)
      if (p < 6 || w == 0) glds16(kvg + c * KV_CH + (w + 4 * p) * 1024, l16, lds0 + (c & 1) * KV_CH + (w + 4 * p) * 1024);
  };
  issue(0);
  const int tok = blk * 8 + 2 * w;
  const f32x16 zero = {0, 0, 0, 0, 0, 0, 0, 0, 0, 0, 0, 0, 0, 0, 0, 0};
  u32x4 q1[2], q2[2]; float gt[2];
#pragma unroll
  for (int t = 0; t < 2; ++t) {
    const char* qp = ws + WS_Q + (size_t)(tok + t) * (D_ * 2) + head * 36;
    q1[t] = *(const u32x4u*)(qp + 16 * h);
    unsigned e = *(const unsigned*)(qp + 32);
    q2[t] = u32x4{h ? 0u : e, 0u, 0u, 0u};
    gt[t] = ((const float*)(ws + WS_GATE))[tok + t];
  }
  wait_vm<0>(); barrier();
  {
    float m[2] = {-3.0e38f, -3.0e38f};
#pragma unroll
    for (int t8 = 0; t8 < 4; ++t8) {
      bf16x8 kf = lds_frag(sm + t8 * 1024 + l16);
      u32x4 k2 = {*(const unsigned*)(sm + 24576 + t8 * 128 + head * 4), 0x3F80u, 0u, 0u};
#pragma unroll
      for (int t = 0; t < 2; ++t) m[t] = max16(MFMA32(as_frag(k2), as_frag(q2[t]), MFMA32(kf, as_frag(q1[t]), zero)), m[t]);
    }
#pragma unroll
    for (int t = 0; t < 2; ++t) {
      float mm = fmaxf(m[t], __shfl_xor(m[t], 32));
      q2[t][1] = h ? 0u : (cvtpk(-mm, 0.f) & 0xffffu);
    }
  }
  f32x16 o[2] = {zero, zero};
  for (int c = 0; c < 8; ++c) {
    if (c) { wait_vm<0>(); wait_lgkm0(); barrier(); }
    if (c < 7) issue(c + 1);
    const char* ch = sm + (c & 1) * KV_CH;
#pragma unroll
    for (int t8 = 0; t8 < 8; ++t8) {
      bf16x8 kf = lds_frag(ch + t8 * 1024 + l16);
      u32x4 k2 = {*(const unsigned*)(ch + 24576 + t8 * 128 + head * 4), 0x3F80u, 0u, 0u};
      bf16x8 v0 = lds_frag(ch + 8192 + t8 * 2048 + l16), v1 = lds_frag(ch + 9216 + t8 * 2048 + l16);
#pragma unroll
      for (int t = 0; t < 2; ++t) {
        f32x16 s = MFMA32(as_frag(k2), as_frag(q2[t]), MFMA32(kf, as_frag(q1[t]), zero));
        pv_step(s, 0.f, v0, v1, o[t]);
      }
    }
  }
  bool ok = true;
#pragma unroll
  for (int t = 0; t < 2; ++t) {
    ok &= o[t][10] >= 0x1p-20f;
#pragma unroll
    for (int r = 0; r < 11; ++r) ok &= fabsf(o[t][r]) <= 0x1p120f;
  }
  if (!__all(ok)) {
    float m[2] = {-3.0e38f, -3.0e38f};
    q2[0][1] = 0u; q2[1][1] = 0u;
    for (int tl = 0; tl < 64; ++tl) {
      const char* ch = kvg + (tl >> 3) * KV_CH;
      bf16x8 kf = *(const bf16x8*)(ch + (tl & 7) * 1024 + l16);
      u32x4 k2 = {*(const unsigned*)(ch + 24576 + (tl & 7) * 128 + head * 4), 0u, 0u, 0u};
#pragma unroll
      for (int t = 0; t < 2; ++t) m[t] = max16(MFMA32(as_frag(k2), as_frag(q2[t]), MFMA32(kf, as_frag(q1[t]), zero)), m[t]);
    }
#pragma unroll
    for (int t = 0; t < 2; ++t) { m[t] = fmaxf(m[t], __shfl_xor(m[t], 32)); o[t] = zero; }
    for (int tl = 0; tl < 64; ++tl) {
      const char* ch = kvg + (tl >> 3) * KV_CH;
      bf16x8 kf = *(const bf16x8*)(ch + (tl & 7) * 1024 + l16);
      u32x4 k2 = {*(const unsigned*)(ch + 24576 + (tl & 7) * 128 + head * 4), 0u, 0u, 0u};
      bf16x8 v0 = *(const bf16x8*)(ch + 8192 + (tl & 7) * 2048 + l16), v1 = *(const bf16x8*)(ch + 9216 + (tl & 7) * 2048 + l16);
#pragma unroll
      for (int t = 0; t < 2; ++t) {
        f32x16 s = MFMA32(as_frag(k2), as_frag(q2[t]), MFMA32(kf, as_frag(q1[t]), zero));
        pv_step(s, m[t], v0, v1, o[t]);
      }
    }
  }
#pragma unroll
  for (int t = 0; t < 2; ++t) {
    const int tk = tok + t, tb = 2 * (tk >> 5) + ((tk >> 2) & 1), i = 4 * ((tk >> 3) & 3) + (tk & 3);
    const float sc = gt[t] / o[t][10];
    char* ub = ws + WS_U + ((size_t)tb * NKB + 72) * 1024 + i * 16;
    u32x4 v;
#pragma unroll
    for (int q = 0; q < 4; ++q) v[q] = cvtpk(o[t][2 * q] * sc, o[t][2 * q + 1] * sc);
    *(u32x4*)(ub + lane * 256) = v;
    if (h == 0) *(unsigned*)(ub + (64 + (head >> 2)) * 256 + (head & 3) * 4) = cvtpk(o[t][8] * sc, o[t][9] * sc);
  }
}

__global__ __launch_bounds__(256, 2) void k2_mix(const u16* __restrict__ wsp, const u16* __restrict__ bsp, char* __restrict__ ws) {
  extern __shared__ __attribute__((aligned(16))) char sm[];
  if (blockIdx.x < 256) gemm2_part(wsp, bsp, ws, sm, blockIdx.x);
  else attn_part(ws, sm, blockIdx.x - 256);
}

constexpr int K3_LDS = 4 * 18 * 1024;
__global__ __launch_bounds__(256, 1) void k3_out(const char* __restrict__ ws, u16* __restrict__ out) {
  extern __shared__ __attribute__((aligned(16))) char sm[];
  const int lane = threadIdx.x & 63, w = __builtin_amdgcn_readfirstlane(threadIdx.x >> 6);
  const int xcd = blockIdx.x & 7, idx = blockIdx.x >> 3;
  const int tg = xcd * 8 + (idx >> 2), dg = idx & 3;
  const char* up = ws + WS_U + ((size_t)(2 * tg) * NKB + w) * 1024 + lane * 16;
  const char* wp = ws + WS_WC + ((size_t)(9 * dg) * NKB + w) * 1024 + lane * 16;
  f32x4 acc[9][2];
#pragma unroll
  for (int n = 0; n < 9; ++n) { acc[n][0] = f32x4{0, 0, 0, 0}; acc[n][1] = f32x4{0, 0, 0, 0}; }
  bf16x8 st[4][11];
  auto load = [&](int s) {
    const int so = (s < 22 || w < 2) ? s * 4096 : 0;
    st[s & 3][9] = *(const bf16x8*)(up + so); st[s & 3][10] = *(const bf16x8*)(up + NKB * 1024 + so);
#pragma unroll
    for (int n = 0; n < 9; ++n) st[s & 3][n] = *(const bf16x8*)(wp + n * (NKB * 1024) + so);
  };
  load(0); load(1); load(2);
#pragma unroll
  for (int s = 0; s < 23; ++s) {
    if (s + 3 < 23) load(s + 3);
    __builtin_amdgcn_sched_barrier(0);
    if (s < 22 || w < 2) {
#pragma unroll
      for (int n = 0; n < 9; ++n) { acc[n][0] = MFMA16(st[s & 3][n], st[s & 3][9], acc[n][0]); acc[n][1] = MFMA16(st[s & 3][n], st[s & 3][10], acc[n][1]); }
    }
    __builtin_amdgcn_sched_barrier(0);
  }
#pragma unroll
  for (int n = 0; n < 9; ++n)
#pragma unroll
    for (int b = 0; b < 2; ++b) *(f32x4*)(sm + (w * 18 + 2 * n + b) * 1024 + lane * 16) = acc[n][b];
  __syncthreads();
  const int li = lane & 15, g = lane >> 4;
#pragma unroll
  for (int p = 0; p < 5; ++p) {
    const int f = w + 4 * p;
    if (f < 18) {
      f32x4 v = *(const f32x4*)(sm + f * 1024 + lane * 16);
#pragma unroll
      for (int u = 1; u < 4; ++u) v += *(const f32x4*)(sm + (u * 18 + f) * 1024 + lane * 16);
      const int tok = tg * 32 + 8 * (li >> 2) + 4 * (f & 1) + (li & 3);
      u32x2 o; o[0] = cvtpk(v[0], v[1]); o[1] = cvtpk(v[2], v[3]);
      *(u32x2*)(out + (size_t)tok * D_ + 144 * dg + 16 * (f >> 1) + 4 * g) = o;
    }
  }
}

extern "C" void kernel_launch(void* const* d_in, const int* in_sizes, int n_in, void* d_out, int out_size, void* d_ws,
                              size_t ws_size, hipStream_t stream) {
  (void)hipFuncSetAttribute((const void*)k1_proj, hipFuncAttributeMaxDynamicSharedMemorySize, K1_LDS);
  (void)hipFuncSetAttribute((const void*)k2_mix, hipFuncAttributeMaxDynamicSharedMemorySize, K2_LDS);
  (void)hipFuncSetAttribute((const void*)k3_out, hipFuncAttributeMaxDynamicSharedMemorySize, K3_LDS);
  char* ws = (char*)d_ws;
  k1_proj<<<256, 256, K1_LDS, stream>>>((const u16*)d_in[0], (const u16*)d_in[1], (const u16*)d_in[2], (const u16*)d_in[3],
                                        (const u16*)d_in[6], (const u16*)d_in[10], (const u16*)d_in[9], (const u16*)d_in[4], ws);
  k2_mix<<<512, 256, K2_LDS, stream>>>((const u16*)d_in[7], (const u16*)d_in[8], ws);
  k3_out<<<256, 256, K3_LDS, stream>>>(ws, (u16*)d_out);
}
```

```cpp
#include <hip/hip_runtime.h>
#include <hip/hip_bf16.h>
#include <utility>
#pragma clang diagnostic ignored "-Winline-asm"

typedef __attribute__((ext_vector_type(8))) __bf16 bf16x8;
typedef __attribute__((ext_vector_type(4))) short s16x4;
typedef __attribute__((ext_vector_type(8))) short s16x8;
typedef __attribute__((ext_vector_type(4))) float f32x4;
typedef __attribute__((ext_vector_type(16))) float f32x16;
typedef __attribute__((ext_vector_type(4))) unsigned u32x4;
typedef __attribute__((ext_vector_type(2))) unsigned u32x2;
typedef __attribute__((ext_vector_type(2))) float f32x2;
typedef unsigned short u16;
#define LDS_AS __attribute__((address_space(3)))
#define GLB_AS __attribute__((address_space(1)))
#define DEVI __device__ __forceinline__

constexpr int S_ = 2048, D_ = 576, FH = 2304, NKB = 90;
constexpr size_t WS_EPOCH = 0;
constexpr size_t WS_STATS = 4096;
constexpr size_t WS_GATE = WS_STATS + 16 * 16 * 128 * 16;
constexpr size_t WS_KV = WS_GATE + 8192;
constexpr size_t WS_Q = WS_KV + 8 * 25600;
constexpr size_t WS_Z2 = WS_Q + (size_t)S_ * D_ * 2;
constexpr size_t WS_U = WS_Z2 + (size_t)S_ * FH * 2;
constexpr size_t WS_WC = WS_U + (size_t)128 * NKB * 1024;

template <int... I, class F> DEVI void sfor_impl(std::integer_sequence<int, I...>, F&& f) { (f(std::integral_constant<int, I>{}), ...); }
template <int N, class F> DEVI void sfor(F&& f) { sfor_impl(std::make_integer_sequence<int, N>{}, f); }
constexpr size_t WS_FLAG = WS_WC + (size_t)36 * NKB * 1024;
constexpr size_t WS_X = WS_FLAG + 4096;
DEVI unsigned cvtpk(float lo, float hi) {
  typedef __attribute__((ext_vector_type(2))) __bf16 bf16x2;
  bf16x2 v; v[0] = (__bf16)lo; v[1] = (__bf16)hi;
  return __builtin_bit_cast(unsigned, v);
}
DEVI float bflo(unsigned u) { return __uint_as_float(u << 16); }
DEVI float bfhi(unsigned u) { return __uint_as_float(u & 0xffff0000u); }
DEVI float bf2f(u16 u) { return __uint_as_float((unsigned)u << 16); }
DEVI void glds16(const void* sbase, int voff, unsigned lds) {
  asm volatile("s_mov_b32 m0, %2\n\ts_nop 0\n\tglobal_load_lds_dwordx4 %0, %1" :: "v"(voff), "s"(sbase), "s"(lds) : "memory", "m0");
}
DEVI void glds16_if(int cond, const void* sbase, int voff, unsigned lds) {
  asm volatile("s_cmp_eq_u32 %3, 0\n\ts_cbranch_scc1 1f\n\ts_mov_b32 m0, %2\n\ts_nop 0\n\tglobal_load_lds_dwordx4 %0, %1\n1:"
               :: "v"(voff), "s"(sbase), "s"(lds), "s"(cond) : "memory", "m0", "scc");
}
DEVI bf16x8 ldsf(unsigned a) { return *(const LDS_AS bf16x8*)(size_t)a; }
DEVI unsigned ldsw(unsigned a) { return *(const LDS_AS unsigned*)(size_t)a; }
DEVI unsigned opaque(unsigned a) { asm volatile("" : "+v"(a)); return a; }
DEVI unsigned lds_addr(const char* p) { return (unsigned)(size_t)(const LDS_AS char*)p; }
DEVI bf16x8 lds_frag(const char* p) { return *(const bf16x8*)p; }
DEVI bf16x8 lds_tr_frag(const char* p0, const char* p1) {
  s16x4 a = __builtin_amdgcn_ds_read_tr16_b64_v4i16((LDS_AS s16x4*)p0);
  s16x4 b = __builtin_amdgcn_ds_read_tr16_b64_v4i16((LDS_AS s16x4*)p1);
  s16x8 r = __builtin_shufflevector(a, b, 0, 1, 2, 3, 4, 5, 6, 7);
  return __builtin_bit_cast(bf16x8, r);
}
#define MFMA16(a, b, c) __builtin_amdgcn_mfma_f32_16x16x32_bf16(a, b, c, 0, 0, 0)
#define MFMA32(a, b, c) __builtin_amdgcn_mfma_f32_32x32x16_bf16(a, b, c, 0, 0, 0)
template <int N> DEVI void wait_vm() { asm volatile("s_waitcnt vmcnt(%0)" ::"i"(N) : "memory"); }
DEVI void wait_lgkm0() { asm volatile("s_waitcnt lgkmcnt(0)" ::: "memory"); }
DEVI void barrier() { __builtin_amdgcn_s_barrier(); asm volatile("" ::: "memory"); }
DEVI float gelu(float x) {
  const float c0 = -2.0f * 0.7978845608028654f * 1.4426950408889634f, c1 = c0 * 0.044715f;
  float w = x * __builtin_fmaf(x * x, c1, c0);
  return x * __builtin_amdgcn_rcpf(1.0f + __builtin_amdgcn_exp2f(w));
}
DEVI f32x2 gelu2(f32x2 x) {
  const float c0 = -2.0f * 0.7978845608028654f * 1.4426950408889634f, c1 = c0 * 0.044715f;
  f32x2 w = x * __builtin_elementwise_fma(x * x, f32x2{c1, c1}, f32x2{c0, c0});
  f32x2 d = f32x2{__builtin_amdgcn_exp2f(w[0]), __builtin_amdgcn_exp2f(w[1])} + f32x2{1.0f, 1.0f};
  return x * f32x2{__builtin_amdgcn_rcpf(d[0]), __builtin_amdgcn_rcpf(d[1])};
}
DEVI unsigned gelu_pk(float a, float b) { f32x2 v = gelu2(f32x2{a, b}); return cvtpk(v[0], v[1]); }
template <int CTRL> DEVI float dpp_add(float v) {
  return v + __builtin_bit_cast(float, __builtin_amdgcn_update_dpp(0, __builtin_bit_cast(int, v), CTRL, 0xf, 0xf, true));
}
DEVI float row16_sum(float v) {
  v = dpp_add<0xB1>(v); v = dpp_add<0x4E>(v); v = dpp_add<0x141>(v); return dpp_add<0x140>(v);
}
#define STAMP(kern, i)
DEVI int swzf(int r) { return ((r >> 1) & 1) | (((r >> 3) & 3) << 1); }
DEVI int swap23(int r) { return (r & ~12) | ((r & 4) << 1) | ((r & 8) >> 1); }

#define Z2OFF(n) ((n) * 1024 + lane * 16)
constexpr int K1_W = 49152, K1_Q = K1_W + 5 * 18432, K1_LDS = K1_Q + 21504;
#define SB() __builtin_amdgcn_sched_barrier(0)
DEVI bf16x8 ldstr(unsigned a0, unsigned a1) {
  s16x4 a = __builtin_amdgcn_ds_read_tr16_b64_v4i16((LDS_AS s16x4*)(size_t)a0), b = __builtin_amdgcn_ds_read_tr16_b64_v4i16((LDS_AS s16x4*)(size_t)a1);
  return __builtin_bit_cast(bf16x8, __builtin_shufflevector(a, b, 0, 1, 2, 3, 4, 5, 6, 7));
}

constexpr int PFH = 14;
__global__ __launch_bounds__(512, 2) void k1_proj(const u16* __restrict__ wsp, const u16* __restrict__ x, const u16* __restrict__ wq,
                                                  const u16* __restrict__ wk, const u16* __restrict__ wv,
                                                  const u16* __restrict__ win, const u16* __restrict__ gw,
                                                  const u16* __restrict__ wout, const u16* __restrict__ wo,
                                                  char* __restrict__ ws) {
  extern __shared__ __attribute__((aligned(16))) char sm[];
  unsigned ka0, ka1;
  asm volatile("s_load_dword %0, %2, 0x8\n\ts_load_dword %1, %2, 0x48" : "=&s"(ka0), "=&s"(ka1) : "s"(__builtin_amdgcn_kernarg_segment_ptr()));
  SB();
  const int lane = threadIdx.x & 63, w8 = __builtin_amdgcn_readfirstlane(threadIdx.x >> 6), w = w8 & 3, role = w8 >> 2;
  const int xcd = blockIdx.x & 7, idx = blockIdx.x >> 3;
  const int rb = (xcd & 1) * 8 + (idx & 7), j = (xcd >> 1) * 4 + (idx >> 3);
  const int li = lane & 15, g = lane >> 4;
  const unsigned lds0 = lds_addr(sm);
  STAMP(0, 0);
  int xoff[2], woff[3];
#pragma unroll
  for (int q = 0; q < 2; ++q) {
    int row = 16 * w8 + 8 * q + (lane >> 3);
    xoff[q] = (rb * 128 + row) * (D_ * 2) + (((lane & 7) ^ swzf(row)) << 4);
  }
#pragma unroll
  for (int p = 0; p < 3; ++p) {
    const int id = w8 + 8 * p;
    if (id < 18) { int c = 64 * (id < 9 ? id : id - 9) + lane, rho = c / 18; woff[p] = swap23(rho) * (2 * FH * 2) + (c - rho * 18) * 16 + (id < 9 ? FH * 2 : 0); }
    else { int c = 64 * (id - 18) + lane, rho = c / 6; woff[p] = swap23(rho) * (D_ * 2) + (c - rho * 6) * 16; }
  }
  const char* xb = (const char*)x;
  const char* wb = (const char*)win + 288 * j;
  const char* cb = w8 < 2 ? wb : (const char*)wq + 96 * j;
  const int con = __builtin_amdgcn_readfirstlane(((j < 12 ? 31 : 3) >> w8) & 1), cstr = w8 < 2 ? 32 * (2 * FH * 2) : 32 * (D_ * 2), cds = w8 < 2 ? 18432 : 3072;
  const unsigned cdst = lds0 + (w8 < 2 ? K1_W + (w8 + 16) * 1024 : K1_Q + (w8 - 2) * 1024);
  const int rot = 2 * (((rb & 7) + 2 * (j & 3)) % 9);
  auto rk = [&](int hs) { const int r = hs + rot; return r >= 18 ? r - 18 : r; };
  auto issue_x = [&](int n) { glds16(xb + rk(n & ~1) * 64, xoff[n & 1], lds0 + ((n >> 1) % 3) * 16384 + (2 * w8 + (n & 1)) * 1024); };
  auto issue1 = [&](int hs, int p) {
    if (p == 3) { if (hs < 17) issue_x(hs + 1); }
    else if (p < 2) glds16(wb + rk(hs) * (32 * 2 * FH * 2), woff[p], lds0 + K1_W + (hs % 5) * 18432 + (w8 + 8 * p) * 1024);
    else glds16_if(con, cb + rk(hs) * cstr, woff[2], cdst + (hs % 5) * cds);
  };
  if (j >= 12) {
    const u16* sw = j == 12 ? wk : j == 13 ? wv : gw;
#pragma unroll
    for (int p = 0; p < 3; ++p)
      if (w8 + 8 * p < (j < 14 ? 21 : 2)) glds16(sw, min(64 * (w8 + 8 * p) + lane, j < 14 ? 1295 : 71) * 16, lds0 + K1_Q + (w8 + 8 * p) * 1024);
  }
  asm volatile("s_waitcnt lgkmcnt(0)" :: "s"(ka0), "s"(ka1));
  issue_x(0);
#pragma unroll
  for (int hs = 0; hs < 4; ++hs)
#pragma unroll
    for (int p = 3; p >= 0; --p) issue1(hs, p);

  const int xrow = 32 * w + 8 * (li >> 2) + (li & 3);
  const int trrow = 16 * (g >> 1) + 4 * (g & 1) + (li >> 2);
  const unsigned rX0 = opaque(lds0 + xrow * 128 + ((g ^ swzf(xrow)) << 4)), rX1 = opaque(rX0 ^ 64);
  const unsigned rW0 = opaque(lds0 + K1_W + role * 9216 + trrow * 288 + 8 * (li & 3)), rW1 = opaque(rW0 + 3 * 18432);
  f32x4 acc[9][2], a2[3][2];
#pragma unroll
  for (int n = 0; n < 9; ++n) { acc[n][0] = f32x4{0, 0, 0, 0}; acc[n][1] = f32x4{0, 0, 0, 0}; }
#pragma unroll
  for (int n = 0; n < 3; ++n) { a2[n][0] = f32x4{0, 0, 0, 0}; a2[n][1] = f32x4{0, 0, 0, 0}; }
  auto mainloop = [&](auto mode_, auto&& mid) {
    constexpr int MODE = decltype(mode_)::value, NX = MODE == 0 ? 0 : MODE == 1 ? 3 : MODE == 4 ? 1 : 2, NF = 11 + NX;
    const unsigned rQ = opaque(lds0 + K1_Q + (MODE == 1 ? trrow * 96 + 8 * (li & 3) : MODE == 4 ? 16 * g : 8 * g * 36 + 2 * li));
    const unsigned rQ1 = opaque(lds0 + K1_Q + 8 * g * 36 + 32 + 2 * (li & 1));
    bf16x8 fr[2][NF];
    auto rd = [&](int hs, int i) {
      bf16x8& d = fr[hs & 1][i];
      if (i < 2) d = ldsf(((hs & 1) ? rX1 : rX0) + ((hs >> 1) % 3) * 16384 + i * 512);
      else if (i < 11) { const unsigned a = (hs % 5 < 3 ? rW0 + (hs % 5) * 18432 : rW1 + (hs % 5 - 3) * 18432) + (i - 2) * 32; d = ldstr(a, a + 8 * 288); }
      else if (MODE == 1) { const unsigned a = rQ + (hs % 5) * 3072 + (i - 11) * 32; d = ldstr(a, a + 8 * 96); }
      else if (MODE == 4) d = ldsf(rQ + rk(hs) * 64);
      else {
        s16x8 t;
        const unsigned a = opaque((i == 11 ? rQ : rQ1) + rk(hs) * (32 * 36));
#pragma unroll
        for (int e = 0; e < 8; ++e) t[e] = *(const LDS_AS short*)(size_t)(a + e * 36);
        d = __builtin_bit_cast(bf16x8, t);
      }
    };
    wait_vm<MODE == 0 ? 17 : 9>(); barrier();
    STAMP(0, 1);
#pragma unroll
    for (int i = 0; i < 11; ++i) rd(0, i);
    sfor<18>([&](auto h_) {
      constexpr int hs = decltype(h_)::value, c2 = hs + 2 <= 16 ? 3 : hs + 2 == 17 ? 2 : 0, c3 = hs + 3 <= 16 ? 3 : hs + 3 == 17 ? 2 : 0;
      if constexpr (hs < 17) { wait_vm<c2 + c3 + (MODE == 0 && hs <= 2 ? 8 : 0) + (MODE != 0 && (hs == PFH + 1 || hs == PFH + 2) ? 4 : 0)>(); wait_lgkm0(); barrier(); }
      if constexpr (hs == (MODE == 0 ? 8 : PFH)) mid();
      sfor<NF - 2>([&](auto n_) {
        constexpr int n = decltype(n_)::value;
        bf16x8(&f)[NF] = fr[hs & 1];
#pragma unroll
        for (int b = 0; b < 2; ++b) {
          f32x4& c = n < 9 ? acc[n < 9 ? n : 0][b] : a2[n < 9 ? 0 : n - 9][b];
          if (MODE == 0 || (MODE == 3 && n >= 9)) c = MFMA16(f[b], f[2 + n], c); else c = MFMA16(f[2 + n], f[b], c);
          if (2 * n + b < 11) { if (hs < 17) rd(hs + 1, 2 * n + b); } else if (2 * n + b < NF) rd(hs, 2 * n + b);
          SB();
        }
        if constexpr (hs + 4 < 18 && n >= 5 && n < 9) issue1(hs + 4, n - 5);
        SB();
      });
    });
  };
  const int tok0 = rb * 128 + 32 * w;
  if (role == 0) {
    GLB_AS unsigned* epoch_p = (GLB_AS unsigned*)(ws + WS_EPOCH + rb * 128);
    unsigned epoch;
    asm volatile("s_load_dword %0, %1, 0x0" : "=s"(epoch) : "s"(epoch_p) : "memory");
    const int item = min(blockIdx.x * 256 + threadIdx.x, NKB * 4 * 144 - 1);
    const int kg = item / 144, dq = item - kg * 144, kb = kg >> 2, gg = kg & 3;
    u32x2 wv8[8];
    {
      const int m = kb / 9, r = kb - 9 * m, c = kg - 288;
      const int base = kb < 72 ? (r < 8 ? 144 * (2 * m + (r >> 2)) + 32 * (r & 3) + 4 * gg : 288 * m + 128 + 4 * gg) : (c < 64 ? (c & 31) * 18 + 4 * (c >> 5) : 72 * (c - 64) + 16);
      const int s4 = kb < 72 ? (r < 8 ? 16 : 144) : (c < 64 ? 8 : 36), s2 = (kb >= 72 && c >= 64) ? 18 : 2;
      const u16* rp = (kb < 72 ? wout : wo) + base * D_ + 4 * dq;
#pragma unroll
      for (int e = 0; e < 8; ++e)
        asm volatile("global_load_dwordx2 %0, %1, off" : "=v"(wv8[e]) : "v"(rp + (s4 * (e >> 2) + s2 * ((e >> 1) & 1) + (e & 1)) * D_) : "memory");
    }
    mainloop(std::integral_constant<int, 0>{}, [&] {
      asm volatile("" : "+v"(wv8[0]), "+v"(wv8[1]), "+v"(wv8[2]), "+v"(wv8[3]), "+v"(wv8[4]), "+v"(wv8[5]), "+v"(wv8[6]), "+v"(wv8[7]), "+s"(epoch));
      char* dst = ws + WS_WC + ((size_t)((dq >> 2) * NKB + kb) * 64 + gg * 16 + 4 * (dq & 3)) * 16;
#pragma unroll
      for (int d = 0; d < 4; ++d) {
        u32x4 o;
#pragma unroll
        for (int q = 0; q < 4; ++q)
          o[q] = __builtin_amdgcn_perm(wv8[2 * q + 1][d >> 1], wv8[2 * q][d >> 1], (d & 1) ? 0x07060302u : 0x05040100u);
        *(u32x4*)(dst + d * 16) = o;
      }
      SB();
    });
    STAMP(0, 2);
    __builtin_amdgcn_s_setprio(1);
    f32x2 ssum[4], ssq[4], z2f[9][4];
#pragma unroll
    for (int n = 0; n < 9; ++n)
#pragma unroll
      for (int q = 0; q < 4; ++q) {
        f32x2 v = gelu2(f32x2{acc[n][q >> 1][2 * (q & 1)], acc[n][q >> 1][2 * (q & 1) + 1]});
        z2f[n][q] = v;
        if (n == 0) { ssum[q] = v; ssq[q] = v * v; } else { ssum[q] += v; ssq[q] = __builtin_elementwise_fma(v, v, ssq[q]); }
      }
    const unsigned tag = epoch + 0x9E3779B9u;
    float pa = 0.f;
#pragma unroll
    for (int i = 0; i < 8; ++i) {
      float su = row16_sum(ssum[i >> 1][i & 1]), sq = row16_sum(ssq[i >> 1][i & 1]);
      if ((li >> 1) == i) pa = (li & 1) ? sq : su;
    }
    GLB_AS unsigned long long* st = (GLB_AS unsigned long long*)(ws + WS_STATS + ((size_t)(rb * 16) * 128 + 32 * w) * 16) + lane;
    __hip_atomic_store(st + j * 256, ((unsigned long long)tag << 32) | __float_as_uint(pa), __ATOMIC_RELAXED, __HIP_MEMORY_SCOPE_AGENT);
    STAMP(0, 3);
    __builtin_amdgcn_s_setprio(0);
    float tot;
    for (int spin = 0; spin < (1 << 18); ++spin) {
      bool ok = true; tot = 0.f;
#pragma unroll
      for (int p = 0; p < 16; ++p) {
        unsigned long long v = __hip_atomic_load(st + p * 256, __ATOMIC_RELAXED, __HIP_MEMORY_SCOPE_AGENT);
        ok &= (unsigned)(v >> 32) == tag; tot += __uint_as_float((unsigned)v);
      }
      if (__all(ok)) break;
      __builtin_amdgcn_s_sleep(1);
    }
    STAMP(0, 4);
    float other = __shfl_xor(tot, 1);
    float sum = (lane & 1) ? other : tot, sq = (lane & 1) ? tot : other;
    float mean = sum * (1.0f / FH);
    float var = fmaxf(sq * (1.0f / FH) - mean * mean, 0.f);
    float rstd = 1.0f / sqrtf(var + 1e-5f);
    float mine = (lane & 1) ? -mean * rstd : rstd;
    f32x2 rs[4], nm[4];
#pragma unroll
    for (int i = 0; i < 8; ++i) { rs[i >> 1][i & 1] = __shfl(mine, 16 * g + 2 * i); nm[i >> 1][i & 1] = __shfl(mine, 16 * g + 2 * i + 1); }
    char* zb = ws + WS_Z2 + ((size_t)(j * 64 + rb * 4 + w) * 9) * 1024;
#pragma unroll
    for (int n = 0; n < 9; ++n) {
      u32x4 o;
#pragma unroll
      for (int q = 0; q < 4; ++q) { f32x2 v = __builtin_elementwise_fma(z2f[n][q], rs[q], nm[q]); o[q] = cvtpk(v[0], v[1]); }
      *(u32x4*)(zb + Z2OFF(n)) = o;
    }
    if (j == 0 && threadIdx.x == 0) __hip_atomic_store(epoch_p, epoch + 1, __ATOMIC_RELAXED, __HIP_MEMORY_SCOPE_AGENT);
    STAMP(0, 5);
  } else {
    unsigned pf[4];
    auto touch = [&] {
      const char* pb = (const char*)wsp + (size_t)(rb * 128 + w * 32) * (S_ * 2) + j * 256;
      const int po = (lane >> 3) * (S_ * 2) + (lane & 7) * 32;
#pragma unroll
      for (int i = 0; i < 4; ++i) asm volatile("global_load_dword %0, %1, %2" : "=v"(pf[i]) : "v"(po), "s"(pb + i * 8 * (S_ * 2)) : "memory");
    };
    if (j < 12) mainloop(std::integral_constant<int, 1>{}, touch);
    else if (j == 12) mainloop(std::integral_constant<int, 2>{}, touch);
    else if (j == 13) mainloop(std::integral_constant<int, 3>{}, touch);
    else mainloop(std::integral_constant<int, 4>{}, touch);
    STAMP(0, 2);
    if (j < 12) {
      const float qs = 0.23570226039551584f * 1.4426950408889634f;
#pragma unroll
      for (int n = 0; n < 3; ++n)
#pragma unroll
        for (int b = 0; b < 2; ++b)
          *(u32x2*)(ws + WS_Q + (size_t)(rb * 128 + xrow + 4 * b) * (D_ * 2) + (48 * j + 16 * n + 4 * g) * 2) =
              u32x2{cvtpk(a2[n][b][0] * qs, a2[n][b][1] * qs), cvtpk(a2[n][b][2] * qs, a2[n][b][3] * qs)};
    } else if (j < 14) {
      const int tile = rb * 4 + w;
      char* kvb = ws + WS_KV + (tile >> 3) * 25600;
#pragma unroll
      for (int b = 0; b < 2; ++b) {
        u32x2 o0 = {cvtpk(a2[0][b][0], a2[0][b][1]), cvtpk(a2[0][b][2], a2[0][b][3])};
        u32x2 o1 = {cvtpk(a2[1][b][0], a2[1][b][1]), cvtpk(a2[1][b][2], a2[1][b][3])};
        if (j == 12) {
          const int key = 8 * (li >> 2) + 4 * b + (li & 3);
          *(u32x2*)(kvb + (tile & 7) * 1024 + ((g >> 1) * 32 + key) * 16 + (g & 1) * 8) = o0;
          if (g == 0) *(unsigned*)(kvb + 24576 + (tile & 7) * 128 + key * 4) = o1[0];
        } else {
          char* vb = kvb + 8192 + (tile & 7) * 2048 + (g >> 1) * 1024 + b * 512 + (g & 1) * 8;
          *(u32x2*)(vb + li * 16) = o0;
          if (li >= 2) { unsigned c = (li == 2 || li == 6) ? 0x3F803F80u : 0u; o1[0] = c; o1[1] = c; }
          *(u32x2*)(vb + (16 + li) * 16) = o1;
        }
      }
    } else if (j == 15) {
      if (w == 0) ((unsigned*)(ws + WS_FLAG))[rb * 64 + lane] = 0u;
    } else if (g < 2) {
      float v = g == 0 ? a2[0][0][0] : a2[0][1][0];
      ((float*)(ws + WS_GATE))[tok0 + 8 * (li >> 2) + 4 * g + (li & 3)] = 1.0f / (1.0f + __expf(-v));
    }
    STAMP(0, 3);
    char* ub = ws + WS_U + ((size_t)((rb * 4 + w) * 2) * NKB + 9 * (j >> 1)) * 1024 + lane * 16;
#pragma unroll
    for (int b = 0; b < 2; ++b) {
#pragma unroll
      for (int i = 0; i < 4; ++i)
        *(u32x4*)(ub + (b * NKB + 4 * (j & 1) + i) * 1024) =
            u32x4{gelu_pk(acc[2 * i][b][0], acc[2 * i][b][1]), gelu_pk(acc[2 * i][b][2], acc[2 * i][b][3]),
                  gelu_pk(acc[2 * i + 1][b][0], acc[2 * i + 1][b][1]), gelu_pk(acc[2 * i + 1][b][2], acc[2 * i + 1][b][3])};
      *(u32x2*)(ub + (b * NKB + 8) * 1024 + (j & 1) * 8) = u32x2{gelu_pk(acc[8][b][0], acc[8][b][1]), gelu_pk(acc[8][b][2], acc[8][b][3])};
    }
    asm volatile("s_waitcnt vmcnt(0)" :: "v"(pf[0]), "v"(pf[1]), "v"(pf[2]), "v"(pf[3]) : "memory");
    STAMP(0, 4); STAMP(0, 5);
  }
}

constexpr int K2_SLOT = 34816, K2_LDS = 2 * K2_SLOT, KV_CH = 25600;
typedef __attribute__((ext_vector_type(4), aligned(4))) unsigned u32x4u;

DEVI void gemm2_part(const u16* __restrict__ wsp, const u16* __restrict__ bsp, char* __restrict__ ws, char* sm, int blk) {
  const int lane = threadIdx.x & 63, w = __builtin_amdgcn_readfirstlane(threadIdx.x >> 6);
  const int xcd = blk & 7, idx = blk >> 3;
  const int rb = (xcd & 1) * 8 + (idx & 7), j = (xcd >> 1) * 4 + (idx >> 3);
  const int li = lane & 15, g = lane >> 4;
  const unsigned lds0 = lds_addr(sm);
  int boff[4];
#pragma unroll
  for (int q = 0; q < 4; ++q) {
    int row = 32 * w + 8 * q + (lane >> 3);
    boff[q] = row * (S_ * 2) + (((lane & 7) ^ swzf(row)) << 4);
  }
  const char* ab = ws + WS_Z2 + (size_t)j * 64 * 9216 + w * 1024;
  const char* bb = (const char*)wsp + (size_t)rb * 128 * (S_ * 2);
  const int l16 = lane * 16;
  auto issue1 = [&](int s, int par, int p) {
    const unsigned slot = lds0 + par * K2_SLOT;
    if (p >= 5) glds16(bb + s * 128, boff[p - 5], slot + (4 * w + p - 5) * 1024);
    else if (p < 4 || w < 2) glds16(ab + s * 18432 + p * 4096, l16, slot + 16384 + (w + 4 * p) * 1024);
  };
  STAMP(1, 0);
#pragma unroll
  for (int p = 0; p < 9; ++p) issue1(0, 0, p);
#pragma unroll
  for (int p = 0; p < 9; ++p) issue1(1, 1, p);
  f32x4 acc[9][2];
#pragma unroll
  for (int n = 0; n < 9; ++n) { acc[n][0] = f32x4{0, 0, 0, 0}; acc[n][1] = f32x4{0, 0, 0, 0}; }
  const int xrow = 32 * w + 8 * (li >> 2) + (li & 3);
  const int xrd = xrow * 128, xsw = swzf(xrow);
  bf16x8 fr[2][11];
  auto rd = [&](int par, int ks, int i) {
    const char* slot = sm + par * K2_SLOT;
    fr[ks][i] = i < 2 ? lds_frag(slot + xrd + 512 * i + (((4 * ks + g) ^ xsw) << 4)) : lds_frag(slot + 16384 + (ks * 9 + i - 2) * 1024 + l16);
  };
  auto half = [&](int par, int ks, int s, bool more, bool refill) {
#pragma unroll
    for (int n = 0; n < 9; ++n) {
      acc[n][0] = MFMA16(fr[ks][2 + n], fr[ks][0], acc[n][0]);
      if (more && 2 * n < 11) rd(ks ? par ^ 1 : par, ks ^ 1, 2 * n);
      __builtin_amdgcn_sched_barrier(0);
      acc[n][1] = MFMA16(fr[ks][2 + n], fr[ks][1], acc[n][1]);
      if (more && 2 * n + 1 < 11) rd(ks ? par ^ 1 : par, ks ^ 1, 2 * n + 1);
      if (refill) issue1(s + 2, par, n);
      __builtin_amdgcn_sched_barrier(0);
    }
  };
  wait_vm<8>(); barrier();
  STAMP(1, 1);
#pragma unroll
  for (int i = 0; i < 11; ++i) rd(0, 0, i);
  for (int s = 0; s < 30; s += 2) {
    if (s == 16) STAMP(1, 2);
    if (s == 12) __builtin_amdgcn_s_setprio(0);
    half(0, 0, s, true, false);
    wait_vm<0>(); wait_lgkm0(); barrier();
    half(0, 1, s, true, true);
    half(1, 0, s + 1, true, false);
    wait_vm<0>(); wait_lgkm0(); barrier();
    half(1, 1, s + 1, true, true);
  }
  half(0, 0, 30, true, false);
  wait_vm<0>(); barrier();
  STAMP(1, 3);
  char* ub = ws + WS_U + ((size_t)((rb * 4 + w) * 2) * NKB + 9 * (j >> 1)) * 1024 + l16;
  u32x4 z1[2][4]; u32x2 z1o[2]; float bs[2], og[2];
#pragma unroll
  for (int b = 0; b < 2; ++b) {
#pragma unroll
    for (int i = 0; i < 4; ++i) z1[b][i] = *(const u32x4*)(ub + (b * NKB + 4 * (j & 1) + i) * 1024);
    z1o[b] = *(const u32x2*)(ub + (b * NKB + 8) * 1024 + (j & 1) * 8);
    const int tok = rb * 128 + xrow + 4 * b;
    bs[b] = bf2f(bsp[tok]); og[b] = ((const float*)(ws + WS_GATE))[tok];
  }
  __builtin_amdgcn_sched_barrier(0);
  half(0, 1, 30, true, false);
  half(1, 0, 31, true, false);
  half(1, 1, 31, false, false);
#pragma unroll
  for (int b = 0; b < 2; ++b) {
    const float sc = 1.0f - og[b], bo = bs[b] * sc;
    auto hp = [&](unsigned z, float a0, float a1) { return cvtpk(bflo(z) * __builtin_fmaf(a0, sc, bo), bfhi(z) * __builtin_fmaf(a1, sc, bo)); };
#pragma unroll
    for (int i = 0; i < 4; ++i) {
      u32x4 o;
      o[0] = hp(z1[b][i][0], acc[2 * i][b][0], acc[2 * i][b][1]); o[1] = hp(z1[b][i][1], acc[2 * i][b][2], acc[2 * i][b][3]);
      o[2] = hp(z1[b][i][2], acc[2 * i + 1][b][0], acc[2 * i + 1][b][1]); o[3] = hp(z1[b][i][3], acc[2 * i + 1][b][2], acc[2 * i + 1][b][3]);
      *(u32x4*)(ub + (b * NKB + 4 * (j & 1) + i) * 1024) = o;
    }
    u32x2 o; o[0] = hp(z1o[b][0], acc[8][b][0], acc[8][b][1]); o[1] = hp(z1o[b][1], acc[8][b][2], acc[8][b][3]);
    *(u32x2*)(ub + (b * NKB + 8) * 1024 + (j & 1) * 8) = o;
  }
  STAMP(1, 4);
}

DEVI bf16x8 as_frag(u32x4 v) { return __builtin_bit_cast(bf16x8, v); }
DEVI void pv_step(const f32x16& s, float sub, bf16x8 v0, bf16x8 v1, f32x16& o) {
  u32x4 p0, p1;
#pragma unroll
  for (int q = 0; q < 4; ++q) {
    p0[q] = cvtpk(__builtin_amdgcn_exp2f(s[2 * q] - sub), __builtin_amdgcn_exp2f(s[2 * q + 1] - sub));
    p1[q] = cvtpk(__builtin_amdgcn_exp2f(s[8 + 2 * q] - sub), __builtin_amdgcn_exp2f(s[9 + 2 * q] - sub));
  }
  o = MFMA32(v0, as_frag(p0), o); o = MFMA32(v1, as_frag(p1), o);
}
DEVI float max16(const f32x16& s, float m) {
#pragma unroll
  for (int r = 0; r < 16; r += 2) m = fmaxf(fmaxf(s[r], s[r + 1]), m);
  return m;
}

DEVI void attn_part(char* __restrict__ ws, char* sm, int blk) {
  const int lane = threadIdx.x & 63, w = __builtin_amdgcn_readfirstlane(threadIdx.x >> 6);
  const int head = lane & 31, h = lane >> 5, l16 = lane * 16;
  const unsigned lds0 = lds_addr(sm);
  const char* kvg = ws + WS_KV;
  auto issue = [&](int c) {
#pragma unroll
    for (int p = 0; p < 7; ++p)
      if (p < 6 || w == 0) glds16(kvg + c * KV_CH + (w + 4 * p) * 1024, l16, lds0 + (c & 1) * KV_CH + (w + 4 * p) * 1024);
  };
  STAMP(1, 0);
  issue(0);
  const int tok = blk * 8 + 2 * w;
  const f32x16 zero = {0, 0, 0, 0, 0, 0, 0, 0, 0, 0, 0, 0, 0, 0, 0, 0};
  u32x4 q1[2], q2[2]; float gt[2];
#pragma unroll
  for (int t = 0; t < 2; ++t) {
    const char* qp = ws + WS_Q + (size_t)(tok + t) * (D_ * 2) + head * 36;
    q1[t] = *(const u32x4u*)(qp + 16 * h);
    unsigned e = *(const unsigned*)(qp + 32);
    q2[t] = u32x4{h ? 0u : e, 0u, 0u, 0u};
    gt[t] = ((const float*)(ws + WS_GATE))[tok + t];
  }
  wait_vm<0>(); barrier();
  STAMP(1, 1);
  {
    float m[2] = {-3.0e38f, -3.0e38f};
#pragma unroll
    for (int t8 = 0; t8 < 4; ++t8) {
      bf16x8 kf = lds_frag(sm + t8 * 1024 + l16);
      u32x4 k2 = {*(const unsigned*)(sm + 24576 + t8 * 128 + head * 4), 0x3F80u, 0u, 0u};
#pragma unroll
      for (int t = 0; t < 2; ++t) m[t] = max16(MFMA32(as_frag(k2), as_frag(q2[t]), MFMA32(kf, as_frag(q1[t]), zero)), m[t]);
    }
#pragma unroll
    for (int t = 0; t < 2; ++t) {
      float mm = fmaxf(m[t], __shfl_xor(m[t], 32));
      q2[t][1] = h ? 0u : (cvtpk(-mm, 0.f) & 0xffffu);
    }
  }
  f32x16 o[2] = {zero, zero};
  {
    bf16x8 kf[2], v0[2], v1[2]; u32x4 k2[2] = {u32x4{0u, 0x3F80u, 0u, 0u}, u32x4{0u, 0x3F80u, 0u, 0u}};
    f32x16 sc[2]; u32x4 p0, p1 = {0u, 0u, 0u, 0u};
    auto rdk = [&](const char* ch, int t8) { kf[t8 & 1] = lds_frag(ch + t8 * 1024 + l16); k2[t8 & 1][0] = *(const unsigned*)(ch + 24576 + t8 * 128 + head * 4); };
    auto rdv = [&](const char* ch, int t8) { v0[t8 & 1] = lds_frag(ch + 8192 + t8 * 2048 + l16); v1[t8 & 1] = lds_frag(ch + 9216 + t8 * 2048 + l16); };
    auto ex4 = [&](const f32x16& s, int r, u32x4& p) {
      p[(r >> 1) & 3] = cvtpk(__builtin_amdgcn_exp2f(s[r]), __builtin_amdgcn_exp2f(s[r + 1]));
      p[((r >> 1) & 3) + 1] = cvtpk(__builtin_amdgcn_exp2f(s[r + 2]), __builtin_amdgcn_exp2f(s[r + 3]));
    };
    issue(1);
    rdk(sm, 0); rdv(sm, 0); v1[1] = v1[0];
    sc[0] = MFMA32(kf[0], as_frag(q1[0]), MFMA32(as_frag(k2[0]), as_frag(q2[0]), zero));
    for (int c = 0; c < 8; ++c) {
      if (c == 4) STAMP(1, 2);
      const char* ch = sm + (c & 1) * KV_CH;
      const char* chn = sm + ((c + 1) & 1) * KV_CH;
#pragma unroll
      for (int u = 0; u < 16; ++u) {
        const int t = u & 1, t8 = u >> 1, tn = (u + 1) >> 1;
        if (u == 14) { wait_vm<0>(); wait_lgkm0(); barrier(); }
        sc[t ^ 1] = MFMA32(as_frag(k2[tn & 1]), as_frag(q2[t ^ 1]), zero);
        if (t == 0) { if (u < 14) rdk(ch, t8 + 1); else rdk(chn, 0); }
        ex4(sc[t], 0, p0);
        __builtin_amdgcn_sched_barrier(0);
        o[t ^ 1] = MFMA32(v1[(t ? t8 : t8 + 1) & 1], as_frag(p1), o[t ^ 1]);
        if (t == 0) { if (u < 14) rdv(ch, t8 + 1); else rdv(chn, 0); }
        ex4(sc[t], 4, p0);
        __builtin_amdgcn_sched_barrier(0);
        sc[t ^ 1] = MFMA32(kf[tn & 1], as_frag(q1[t ^ 1]), sc[t ^ 1]);
        if (u == 14 && c < 6) issue(c + 2);
        ex4(sc[t], 8, p1);
        __builtin_amdgcn_sched_barrier(0);
        o[t] = MFMA32(v0[t8 & 1], as_frag(p0), o[t]);
        ex4(sc[t], 12, p1);
        __builtin_amdgcn_sched_barrier(0);
      }
    }
    o[1] = MFMA32(v1[1], as_frag(p1), o[1]);
  }
  STAMP(1, 3);
  bool ok = true;
#pragma unroll
  for (int t = 0; t < 2; ++t) {
    ok &= o[t][10] >= 0x1p-20f;
#pragma unroll
    for (int r = 0; r < 11; ++r) ok &= fabsf(o[t][r]) <= 0x1p120f;
  }
  if (!__all(ok)) {
    float m[2] = {-3.0e38f, -3.0e38f};
    q2[0][1] = 0u; q2[1][1] = 0u;
    for (int tl = 0; tl < 64; ++tl) {
      const char* ch = kvg + (tl >> 3) * KV_CH;
      bf16x8 kf = *(const bf16x8*)(ch + (tl & 7) * 1024 + l16);
      u32x4 k2 = {*(const unsigned*)(ch + 24576 + (tl & 7) * 128 + head * 4), 0u, 0u, 0u};
#pragma unroll
      for (int t = 0; t < 2; ++t) m[t] = max16(MFMA32(as_frag(k2), as_frag(q2[t]), MFMA32(kf, as_frag(q1[t]), zero)), m[t]);
    }
#pragma unroll
    for (int t = 0; t < 2; ++t) { m[t] = fmaxf(m[t], __shfl_xor(m[t], 32)); o[t] = zero; }
    for (int tl = 0; tl < 64; ++tl) {
      const char* ch = kvg + (tl >> 3) * KV_CH;
      bf16x8 kf = *(const bf16x8*)(ch + (tl & 7) * 1024 + l16);
      u32x4 k2 = {*(const unsigned*)(ch + 24576 + (tl & 7) * 128 + head * 4), 0u, 0u, 0u};
      bf16x8 v0 = *(const bf16x8*)(ch + 8192 + (tl & 7) * 2048 + l16), v1 = *(const bf16x8*)(ch + 9216 + (tl & 7) * 2048 + l16);
#pragma unroll
      for (int t = 0; t < 2; ++t) {
        f32x16 s = MFMA32(as_frag(k2), as_frag(q2[t]), MFMA32(kf, as_frag(q1[t]), zero));
        pv_step(s, m[t], v0, v1, o[t]);
      }
    }
  }
#pragma unroll
  for (int t = 0; t < 2; ++t) {
    const int tk = tok + t, tb = 2 * (tk >> 5) + ((tk >> 2) & 1), i = 4 * ((tk >> 3) & 3) + (tk & 3);
    const float sc = gt[t] / o[t][10];
    char* ub = ws + WS_U + ((size_t)tb * NKB + 72) * 1024 + i * 16;
    u32x4 v;
#pragma unroll
    for (int q = 0; q < 4; ++q) v[q] = cvtpk(o[t][2 * q] * sc, o[t][2 * q + 1] * sc);
    *(u32x4*)(ub + lane * 256) = v;
    if (h == 0) *(unsigned*)(ub + (64 + (head >> 2)) * 256 + (head & 3) * 4) = cvtpk(o[t][8] * sc, o[t][9] * sc);
  }
  STAMP(1, 4);
}

__global__ __launch_bounds__(256, 2) void k2_mix(const u16* __restrict__ wsp, const u16* __restrict__ bsp, char* __restrict__ ws) {
  extern __shared__ __attribute__((aligned(16))) char sm[];
  if (blockIdx.x < 256) { __builtin_amdgcn_s_setprio(2); gemm2_part(wsp, bsp, ws, sm, blockIdx.x); }
  else { __builtin_amdgcn_s_setprio(1); attn_part(ws, sm, blockIdx.x - 256); }
}

constexpr int K3_SLOT = 17408, K3_LDS = 8 * K3_SLOT;
__global__ __launch_bounds__(256, 1) void k3_out(char* __restrict__ ws, u16* __restrict__ out) {
  extern __shared__ __attribute__((aligned(16))) char sm[];
  const int lane = threadIdx.x & 63, w = __builtin_amdgcn_readfirstlane(threadIdx.x >> 6), l16 = lane * 16;
  const int xcd = blockIdx.x & 7, idx = blockIdx.x >> 3;
  const int kq = xcd >> 1, rb = (xcd & 1) * 8 + (idx & 7), dg = idx >> 3;
  const unsigned lds0 = lds_addr(sm);
  const char* up = ws + WS_U + (size_t)(rb * 8 + 2 * w) * (NKB * 1024);
  const char* wp = ws + WS_WC + (size_t)(9 * dg + 2 * w) * (NKB * 1024);
  const int ob = 72 - 18 + (kq < 2 ? 5 * kq : 4 * kq + 2), last = (w + 1) >> 2;
  auto issue1 = [&](int s, int p) {
    const int kb = (s < 18 ? 18 * kq + s : ob + ((s == 22 && kq >= 2) ? 21 : s)) * 1024;
    const unsigned slot = lds0 + (s & 7) * K3_SLOT;
    if (p < 2) glds16(up + p * (NKB * 1024) + kb, l16, slot + (2 * w + p) * 1024);
    else if (p < 4) glds16(wp + (p - 2) * (NKB * 1024) + kb, l16, slot + 8192 + (2 * w + p - 2) * 1024);
    else glds16_if(last, wp + 2 * (NKB * 1024) + kb, l16, slot + 16384);
  };
  STAMP(2, 0);
#pragma unroll
  for (int s = 0; s < 6; ++s)
#pragma unroll
    for (int p = 0; p < 5; ++p) issue1(s, p);
  f32x4 acc[9][2];
#pragma unroll
  for (int n = 0; n < 9; ++n) { acc[n][0] = f32x4{0, 0, 0, 0}; acc[n][1] = f32x4{0, 0, 0, 0}; }
  unsigned rA[3], rB[3];
#pragma unroll
  for (int i = 0; i < 3; ++i) { rA[i] = opaque(lds0 + 3 * i * K3_SLOT + 8192 + l16); rB[i] = opaque(lds0 + 3 * i * K3_SLOT + 2048 * w + l16); }
  bf16x8 fr[2][11];
  auto rd = [&](int s, int i) { fr[s & 1][i] = ldsf((i < 2 ? rB[(s & 7) / 3] + i * 1024 : rA[(s & 7) / 3] + (i - 2) * 1024) + ((s & 7) % 3) * K3_SLOT); };
  wait_vm<20>(); barrier();
  STAMP(2, 5);
#pragma unroll
  for (int i = 0; i < 11; ++i) rd(0, i);
  sfor<23>([&](auto s_) {
    constexpr int s = decltype(s_)::value, y = 21 - s < 4 ? (21 - s < 0 ? 0 : 21 - s) : 4;
    if constexpr (s < 22) { wait_vm<4 * y>(); barrier(); }
    if (s < 22 || kq < 2) {
      sfor<9>([&](auto n_) {
        constexpr int n = decltype(n_)::value;
#pragma unroll
        for (int b = 0; b < 2; ++b) {
          acc[n][b] = MFMA16(fr[s & 1][2 + n], fr[s & 1][b], acc[n][b]);
          if (s < 22 && 2 * n + b < 11) rd(s + 1, 2 * n + b);
          SB();
        }
        if constexpr (s + 6 < 23 && n >= 4) issue1(s + 6, n - 4);
        SB();
      });
    }
  });
  STAMP(2, 1);
  char* xb = ws + WS_X + (size_t)((rb * 4 + dg) * 16) * 9216;
  GLB_AS unsigned* fl = (GLB_AS unsigned*)(ws + WS_FLAG) + (rb * 4 + dg) * 16;
  if (w == kq) {
#pragma unroll
    for (int f = 0; f < 18; ++f) *(f32x4*)(sm + f * 1024 + l16) = acc[f >> 1][f & 1];
  } else {
#pragma unroll
    for (int n = 0; n < 9; ++n) {
      u32x4 v = {cvtpk(acc[n][0][0], acc[n][0][1]), cvtpk(acc[n][0][2], acc[n][0][3]), cvtpk(acc[n][1][0], acc[n][1][1]), cvtpk(acc[n][1][2], acc[n][1][3])};
      asm volatile("global_store_dwordx4 %0, %1, %2 sc1\n\ts_nop 1" :: "v"(l16 + n * 1024), "v"(v), "s"(xb + (w * 4 + kq) * 9216) : "memory");
    }
    wait_vm<0>();
    if (lane == 0) __hip_atomic_store(fl + w * 4 + kq, 1u, __ATOMIC_RELAXED, __HIP_MEMORY_SCOPE_AGENT);
  }
  STAMP(2, 3);
  wait_lgkm0(); barrier();
  for (int spin = 0; spin < (1 << 18); ++spin) {
    unsigned v = __hip_atomic_load(fl + kq * 4 + (lane & 3), __ATOMIC_RELAXED, __HIP_MEMORY_SCOPE_AGENT);
    if (__all(v == 1u || (lane & 3) == kq)) break;
    __builtin_amdgcn_s_sleep(1);
  }
  STAMP(2, 4);
  u32x4 pt[3][3];
#pragma unroll
  for (int p = 0; p < 3; ++p)
#pragma unroll
    for (int u = 0; u < 3; ++u)
      asm volatile("global_load_dwordx4 %0, %1, %2 sc1" : "=v"(pt[p][u]) : "v"(l16 + min(w + 4 * p, 8) * 1024), "s"(xb + (kq * 4 + ((kq + 1 + u) & 3)) * 9216) : "memory");
  const int li = lane & 15, g = lane >> 4;
  sfor<3>([&](auto p_) {
    constexpr int p = decltype(p_)::value;
    const int n = w + 4 * p;
    asm volatile("s_waitcnt vmcnt(%3)" : "+v"(pt[p][0]), "+v"(pt[p][1]), "+v"(pt[p][2]) : "i"(6 - 3 * p) : "memory");
    if (n < 9) {
#pragma unroll
      for (int b = 0; b < 2; ++b) {
        f32x4 v = *(const f32x4*)(sm + (2 * n + b) * 1024 + l16);
#pragma unroll
        for (int u = 0; u < 3; ++u) v += f32x4{bflo(pt[p][u][2 * b]), bfhi(pt[p][u][2 * b]), bflo(pt[p][u][2 * b + 1]), bfhi(pt[p][u][2 * b + 1])};
        const int tok = rb * 128 + kq * 32 + 8 * (li >> 2) + 4 * b + (li & 3);
        *(u32x2*)(out + (size_t)tok * D_ + 144 * dg + 16 * n + 4 * g) = u32x2{cvtpk(v[0], v[1]), cvtpk(v[2], v[3])};
      }
    }
  });
  STAMP(2, 2);
}

extern "C" void kernel_launch(void* const* d_in, const int* in_sizes, int n_in, void* d_out, int out_size, void* d_ws,
                              size_t ws_size, hipStream_t stream) {
  (void)hipFuncSetAttribute((const void*)k1_proj, hipFuncAttributeMaxDynamicSharedMemorySize, K1_LDS);
  (void)hipFuncSetAttribute((const void*)k2_mix, hipFuncAttributeMaxDynamicSharedMemorySize, K2_LDS);
  (void)hipFuncSetAttribute((const void*)k3_out, hipFuncAttributeMaxDynamicSharedMemorySize, K3_LDS);
  char* ws = (char*)d_ws;
  k1_proj<<<256, 512, K1_LDS, stream>>>((const u16*)d_in[7], (const u16*)d_in[0], (const u16*)d_in[1], (const u16*)d_in[2], (const u16*)d_in[3],
                                        (const u16*)d_in[6], (const u16*)d_in[10], (const u16*)d_in[9], (const u16*)d_in[4], ws);
  k2_mix<<<512, 256, K2_LDS, stream>>>((const u16*)d_in[7], (const u16*)d_in[8], ws);
  k3_out<<<256, 256, K3_LDS, stream>>>(ws, (u16*)d_out);
}
```
